```python
import torch

def _softmax(x, axis=-1):
    return torch.softmax(x, dim=axis)
def _gelu(x):
    return torch.nn.functional.gelu(x, approximate="tanh")
def _silu(x):
    return torch.nn.functional.silu(x)
def _sigmoid(x):
    return torch.sigmoid(x)
def _layernorm(x, axis=-1, eps=1e-5):
    m = x.mean(dim=axis, keepdim=True)
    v = x.var(dim=axis, keepdim=True, unbiased=False)
    return (x - m) / torch.sqrt(v + eps)
def _scatter_add(tgt, idx, src, axis=0):
    return tgt.clone().index_add_(axis, idx, src)
def _segment_sum(src, seg, n):
    out = torch.zeros((n,) + src.shape[1:], dtype=src.dtype)
    return out.index_add_(0, seg, src)
def _pad(x, pw):
    flat = tuple(v for pair in reversed(pw) for v in pair)
    return torch.nn.functional.pad(x, flat)

kernel_name = 'gated_mix__ssm_conv_h3__dense_activation_mish__prompt'

def setup_inputs():

    _w = (lambda a: (lambda t: t.to(torch.bfloat16) if t.is_floating_point() else t)(torch.as_tensor(a)))
    def _A_setup():
        B, S, D = 1, 2048, 512
        ks = 4
        return {
            "q": _w((lambda *s: torch.randn(*s, dtype=torch.bfloat16) * 0.05)(B, S, D)),
            "k": _w((lambda *s: torch.randn(*s, dtype=torch.bfloat16) * 0.2)(B, S, D)),
            "v": _w((lambda *s: torch.randn(*s, dtype=torch.bfloat16) * 0.2)(B, S, D)),
            "shift_k": _w((lambda *s: torch.randn(*s, dtype=torch.bfloat16) * 0.2)(D, ks) * 0.5),
            "ssm_k": _w((lambda *s: torch.randn(*s, dtype=torch.bfloat16) * 0.8)(D, S) * (1.0 / S ** 0.5)),
            "ssm_D": _w((lambda *s: torch.randn(*s, dtype=torch.bfloat16) * 0.2)(D)),
        }
    _A_in = _A_setup()

    def _B_setup():
        B, S, D, F = 1, 2048, 512, 1376
        return {
            "x": _w((lambda *s: torch.randn(*s, dtype=torch.bfloat16) * 0.05)(B, S, D)),
            "w1": _w((lambda *s: torch.randn(*s, dtype=torch.bfloat16) * 3.2)(D, F) * (D ** -0.5)),
            "b1": _w((lambda *s: torch.randn(*s, dtype=torch.bfloat16) * 0.05)(F)),
            "w2": _w((lambda *s: torch.randn(*s, dtype=torch.bfloat16) * 0.05)(F, D) * (F ** -0.5)),
        }
    _B_in = _B_setup()

    return {
        "A_q": _A_in["q"],
        "A_k": _A_in["k"],
        "A_v": _A_in["v"],
        "A_shift_k": _A_in["shift_k"],
        "A_ssm_k": _A_in["ssm_k"],
        "A_ssm_D": _A_in["ssm_D"],
        "B_x": _B_in["x"],
        "B_w1": _B_in["w1"],
        "B_b1": _B_in["b1"],
        "B_w2": _B_in["w2"],
        "gate_w": _w((lambda *s: torch.randn(*s, dtype=torch.bfloat16) * 0.05)(512) * 16.0),
    }

def reference(A_q, A_k, A_v, A_shift_k, A_ssm_k, A_ssm_D, B_x, B_w1, B_b1, B_w2, gate_w):
    def _A_ref(q, k, v, shift_k, ssm_k, ssm_D):
        B, S, D = q.shape
        ks = shift_k.shape[-1]
        k_pad = _pad(k, ((0, 0), (ks - 1, 0), (0, 0)))
        k_shift = torch.zeros_like(k)
        for t in range(ks):
            k_shift = k_shift + k_pad[:, t:t + S, :] * shift_k[:, ks - 1 - t][None, None, :]
        kv = k_shift * v
        L = 2 * S
        kv_pad = _pad(kv, ((0, 0), (0, S), (0, 0)))
        kf_pad = _pad(ssm_k, ((0, 0), (0, S)))
        kv_f = (lambda x, axes=(-2,-1): torch.fft.rfft2(x.float(), dim=axes))(kv_pad[:, None, :, :], axes=(1, 2))
        kf_f = (lambda x, axes=(-2,-1): torch.fft.rfft2(x.float(), dim=axes))(kf_pad[:, None, :], axes=(1, 2))
        kf_f = (lambda x, *d: x.permute(*d))(kf_f, 1, 2, 0)
        y_f = kv_f * kf_f[None, :, :, :]
        y = (lambda x, s, axes=(-2,-1): torch.fft.irfft2(x, s=s, dim=axes).to(torch.bfloat16))(y_f, (1, L), axes=(1, 2))[:, 0, :S, :]
        y = y + kv * ssm_D[None, None, :]
        return q * y

    def _B_ref(x, w1, b1, w2):
        h = torch.einsum("bsd,df->bsf", x, w1) + b1
        sp = torch.log1p(torch.exp(-torch.abs(h))) + torch.where(h > 0, h, torch.zeros_like(h))
        act = h * torch.tanh(sp)
        return torch.einsum("bsf,fd->bsd", act, w2)

    x = A_q
    g = _sigmoid((x * gate_w).sum(-1, keepdims=True))
    a = _A_ref(x, A_k, A_v, A_shift_k, A_ssm_k, A_ssm_D)
    b = _B_ref(x, B_w1, B_b1, B_w2)
    return g * a + (1 - g) * b
```

```cpp
#include <hip/hip_runtime.h>
#include <hip/hip_bf16.h>

#define S_ 2048
#define D_ 512
#define F_ 1376

static __device__ __forceinline__ float bf2f(const __hip_bfloat16& x) { return __bfloat162float(x); }
static __device__ __forceinline__ float rbf(float x) { return __bfloat162float(__float2bfloat16(x)); }

__global__ void k_gate(const __hip_bfloat16* x, const __hip_bfloat16* gw, float* g) {
  int s = blockIdx.x; int t = threadIdx.x;
  float acc = 0.f;
  for (int d = t; d < D_; d += 64) acc += rbf(bf2f(x[s * D_ + d]) * bf2f(gw[d]));
  for (int o = 32; o > 0; o >>= 1) acc += __shfl_down(acc, o);
  if (t == 0) { float z = rbf(acc); g[s] = rbf(1.f / (1.f + __expf(-z))); }
}
__global__ void k_kv(const __hip_bfloat16* k, const __hip_bfloat16* v, const __hip_bfloat16* sh, float* kv) {
  int idx = blockIdx.x * blockDim.x + threadIdx.x; int s = idx / D_, d = idx % D_;
  float acc = 0.f;
  for (int t = 0; t < 4; ++t) { int ss = s + t - 3; float kk = ss >= 0 ? bf2f(k[ss * D_ + d]) : 0.f; acc = rbf(acc + rbf(kk * bf2f(sh[d * 4 + 3 - t]))); }
  kv[idx] = rbf(acc * bf2f(v[idx]));
}
__global__ void k_conv(const float* kv, const __hip_bfloat16* h, const __hip_bfloat16* Dv, const __hip_bfloat16* q, float* a) {
  int idx = blockIdx.x * blockDim.x + threadIdx.x; int s = idx / D_, d = idx % D_;
  float acc = 0.f;
  for (int u = 0; u <= s; ++u) acc += kv[u * D_ + d] * bf2f(h[d * S_ + (s - u)]);
  float y = rbf(acc); y = rbf(y + rbf(kv[idx] * bf2f(Dv[d])));
  a[idx] = rbf(bf2f(q[idx]) * y);
}
__global__ void k_g1(const __hip_bfloat16* x, const __hip_bfloat16* w1, const __hip_bfloat16* b1, float* act) {
  int idx = blockIdx.x * blockDim.x + threadIdx.x; if (idx >= S_ * F_) return; int s = idx / F_, f = idx % F_;
  float acc = 0.f;
  for (int d = 0; d < D_; ++d) acc += bf2f(x[s * D_ + d]) * bf2f(w1[d * F_ + f]);
  float h = rbf(rbf(acc) + bf2f(b1[f]));
  float sp = rbf(rbf(log1pf(rbf(__expf(-fabsf(h))))) + (h > 0.f ? h : 0.f));
  act[idx] = rbf(h * rbf(tanhf(sp)));
}
__global__ void k_g2(const float* act, const __hip_bfloat16* w2, const float* g, const float* a, __hip_bfloat16* out) {
  int idx = blockIdx.x * blockDim.x + threadIdx.x; int s = idx / D_, d = idx % D_;
  float acc = 0.f;
  for (int f = 0; f < F_; ++f) acc += act[s * F_ + f] * bf2f(w2[f * D_ + d]);
  float b = rbf(acc); float gg = g[s];
  out[idx] = __float2bfloat16(rbf(gg * a[idx]) + rbf(rbf(1.f - gg) * b));
}

extern "C" void kernel_launch(void* const* d_in, const int* in_sizes, int n_in,
                              void* d_out, int out_size, void* d_ws, size_t ws_size,
                              hipStream_t stream) {
  const __hip_bfloat16* q = (const __hip_bfloat16*)d_in[0];
  const __hip_bfloat16* k = (const __hip_bfloat16*)d_in[1];
  const __hip_bfloat16* v = (const __hip_bfloat16*)d_in[2];
  const __hip_bfloat16* sh = (const __hip_bfloat16*)d_in[3];
  const __hip_bfloat16* h = (const __hip_bfloat16*)d_in[4];
  const __hip_bfloat16* Dv = (const __hip_bfloat16*)d_in[5];
  const __hip_bfloat16* w1 = (const __hip_bfloat16*)d_in[7];
  const __hip_bfloat16* b1 = (const __hip_bfloat16*)d_in[8];
  const __hip_bfloat16* w2 = (const __hip_bfloat16*)d_in[9];
  const __hip_bfloat16* gw = (const __hip_bfloat16*)d_in[10];
  float* g = (float*)d_ws; float* kv = g + 4096; float* a = kv + S_ * D_; float* act = a + S_ * D_;
  k_gate<<<S_, 64, 0, stream>>>(q, gw, g);
  k_kv<<<S_ * D_ / 256, 256, 0, stream>>>(k, v, sh, kv);
  k_conv<<<S_ * D_ / 256, 256, 0, stream>>>(kv, h, Dv, q, a);
  k_g1<<<(S_ * F_ + 255) / 256, 256, 0, stream>>>(q, w1, b1, act);
  k_g2<<<S_ * D_ / 256, 256, 0, stream>>>(act, w2, g, a, (__hip_bfloat16*)d_out);
}
```

```cpp
#include <hip/hip_runtime.h>
#include <hip/hip_bf16.h>
#include <stdint.h>

#define S_ 2048
#define D_ 512
#define F_ 1376

typedef __attribute__((ext_vector_type(8))) short bf16x8;
typedef __attribute__((ext_vector_type(4))) short bf16x4;
typedef __attribute__((ext_vector_type(16))) float f32x16;
typedef __attribute__((ext_vector_type(4))) unsigned int u32x4;
typedef __attribute__((ext_vector_type(2))) unsigned int u32x2;
typedef unsigned short u16;

static __device__ __forceinline__ float bf_lo(unsigned int w) { return __uint_as_float(w << 16); }
static __device__ __forceinline__ float bf_hi(unsigned int w) { return __uint_as_float(w & 0xffff0000u); }
static __device__ __forceinline__ float bf1(u16 b) { return __uint_as_float(((unsigned int)b) << 16); }
static __device__ __forceinline__ u16 f2bf(float x) {
  __hip_bfloat16 b = __float2bfloat16(x);
  return *reinterpret_cast<u16*>(&b);
}
static __device__ __forceinline__ float rbf(float x) { return bf1(f2bf(x)); }
static __device__ __forceinline__ unsigned int pack2(float lo, float hi) {
  return (unsigned int)f2bf(lo) | (((unsigned int)f2bf(hi)) << 16);
}

#define STAMP(i)
#define WAIT_VM(n) asm volatile("s_waitcnt vmcnt(" #n ")" ::: "memory")
#define WAIT_LGKM0() asm volatile("s_waitcnt lgkmcnt(0)" ::: "memory")

static __device__ __forceinline__ void glds16(const void* g, unsigned int lds_addr_uniform) {
  unsigned int keep;
  asm volatile("s_mov_b32 %0, m0\n\ts_mov_b32 m0, %2\n\ts_nop 0\n\tglobal_load_lds_dwordx4 %1, off\n\ts_mov_b32 m0, %0"
               : "=&s"(keep) : "v"(g), "s"(lds_addr_uniform) : "memory");
}
static __device__ __forceinline__ unsigned int lds_addr(const void* p) { return (unsigned int)(size_t)p; }
static __device__ __forceinline__ bf16x4 tr_read(const char* p) {
  return __builtin_amdgcn_ds_read_tr16_b64_v4i16(
      (__attribute__((address_space(3))) bf16x4*)(p));
}

#define CV_CS 4160
#define CV_HS 2624
#define CV_XOFF (8 * CV_CS)
#define CV_CH (CV_XOFF + 2 * CV_HS)
#define K1_LDS (4 * CV_CH)
#define G1_STG 40960
#define G1_NST 3

#define WS_ACT 0
#define WS_ZT (WS_ACT + (size_t)S_ * F_ * 2)
#define WS_G (WS_ZT + (size_t)D_ * S_ * 2)

template <int HALF>
static __device__ __forceinline__ void conv_mfma(const char* ap, const char* bp, f32x16* acc) {
  constexpr int CB0 = HALF ? 1 : 0, CB1 = HALF ? 2 : 3;
  constexpr int Q0 = 16 * (CB0 + 1), Q1 = 16 * (CB1 + 1);
  bf16x8 An = *reinterpret_cast<const bf16x8*>(ap);
  bf16x8 B0n = *reinterpret_cast<const bf16x8*>(bp + 512 * CB0);
  bf16x8 B1n = *reinterpret_cast<const bf16x8*>(bp + 512 * CB1);
#pragma unroll
  for (int q = 0; q < Q1; ++q) {
    const bf16x8 Ac = An, B0c = B0n, B1c = B1n;
    if (q + 1 < Q1) {
      An = *reinterpret_cast<const bf16x8*>(ap + 64 * (q + 1));
      if (q + 1 < Q0) B0n = *reinterpret_cast<const bf16x8*>(bp + 512 * CB0 - 32 * (q + 1));
      B1n = *reinterpret_cast<const bf16x8*>(bp + 512 * CB1 - 32 * (q + 1));
    }
    if (q < Q0) acc[0] = __builtin_amdgcn_mfma_f32_32x32x16_bf16(Ac, B0c, acc[0], 0, 0, 0);
    acc[1] = __builtin_amdgcn_mfma_f32_32x32x16_bf16(Ac, B1c, acc[1], 0, 0, 0);
  }
}

static __device__ __forceinline__ void conv_block(int cg, const u16* __restrict__ kp, const u16* __restrict__ vp,
                                                  const u16* __restrict__ shp, const u16* __restrict__ hp,
                                                  const u16* __restrict__ Dp, u16* __restrict__ zT, char* lds) {
  const int tid = threadIdx.x, lane = tid & 63, wv = tid >> 6;
  const int d0 = cg * 4;
  STAMP(0);
  if (tid < 256) {
    u32x2 kr[11], vr[8];
#pragma unroll
    for (int i = 0; i < 11; ++i) {
      const int row = max(8 * tid - 3 + i, 0);
      kr[i] = *reinterpret_cast<const u32x2*>(kp + (size_t)row * D_ + d0);
    }
#pragma unroll
    for (int i = 0; i < 8; ++i) vr[i] = *reinterpret_cast<const u32x2*>(vp + (size_t)(8 * tid + i) * D_ + d0);
    float sh[4][4];
    {
      u32x4 s0 = *reinterpret_cast<const u32x4*>(shp + d0 * 4);
      u32x4 s1 = *reinterpret_cast<const u32x4*>(shp + d0 * 4 + 8);
      sh[0][0] = bf_lo(s0.x); sh[0][1] = bf_hi(s0.x); sh[0][2] = bf_lo(s0.y); sh[0][3] = bf_hi(s0.y);
      sh[1][0] = bf_lo(s0.z); sh[1][1] = bf_hi(s0.z); sh[1][2] = bf_lo(s0.w); sh[1][3] = bf_hi(s0.w);
      sh[2][0] = bf_lo(s1.x); sh[2][1] = bf_hi(s1.x); sh[2][2] = bf_lo(s1.y); sh[2][3] = bf_hi(s1.y);
      sh[3][0] = bf_lo(s1.z); sh[3][1] = bf_hi(s1.z); sh[3][2] = bf_lo(s1.w); sh[3][3] = bf_hi(s1.w);
    }
    {
      const int c = tid >> 6, idx = tid & 63;
      *reinterpret_cast<u32x4*>(lds + c * CV_CH + CV_XOFF + (idx >> 5) * CV_HS + 16 * (idx & 31)) = (u32x4){0u, 0u, 0u, 0u};
    }
    if (tid == 0) {
#pragma unroll
      for (int i = 0; i < 3; ++i) kr[i] = (u32x2){0u, 0u};
    }
    float kf[11][4];
#pragma unroll
    for (int i = 0; i < 11; ++i) { kf[i][0] = bf_lo(kr[i].x); kf[i][1] = bf_hi(kr[i].x); kf[i][2] = bf_lo(kr[i].y); kf[i][3] = bf_hi(kr[i].y); }
    const int Ip = tid >> 1, hsel = (tid & 1) ? 0 : 1;
#pragma unroll
    for (int c = 0; c < 4; ++c) {
      u16 xs[8];
#pragma unroll
      for (int i = 0; i < 8; ++i) {
        float ks = kf[i][c] * sh[c][3];
        ks = fmaf(kf[i + 1][c], sh[c][2], ks);
        ks = fmaf(kf[i + 2][c], sh[c][1], ks);
        ks = fmaf(kf[i + 3][c], sh[c][0], ks);
        float vv = (c == 0) ? bf_lo(vr[i].x) : (c == 1) ? bf_hi(vr[i].x) : (c == 2) ? bf_lo(vr[i].y) : bf_hi(vr[i].y);
        xs[i] = f2bf(rbf(ks) * vv);
      }
      u32x4 o;
      o.x = (unsigned)xs[7] | ((unsigned)xs[6] << 16);
      o.y = (unsigned)xs[5] | ((unsigned)xs[4] << 16);
      o.z = (unsigned)xs[3] | ((unsigned)xs[2] << 16);
      o.w = (unsigned)xs[1] | ((unsigned)xs[0] << 16);
      *reinterpret_cast<u32x4*>(lds + c * CV_CH + CV_XOFF + hsel * CV_HS + 16 * (Ip + 32)) = o;
    }
  } else {
    const int t = tid - 256;
    u32x4 hw[4][2];
#pragma unroll
    for (int c = 0; c < 4; ++c) {
      const u16* hrow = hp + (size_t)(d0 + c) * S_;
      hw[c][0] = *reinterpret_cast<const u32x4*>(hrow + 8 * max(t - 1, 0));
      hw[c][1] = *reinterpret_cast<const u32x4*>(hrow + 8 * t);
    }
    if (t < 32) *reinterpret_cast<u32x4*>(lds + (t >> 3) * CV_CH + (t & 7) * CV_CS) = (u32x4){0u, 0u, 0u, 0u};
    if (t == 0) {
#pragma unroll
      for (int c = 0; c < 4; ++c) hw[c][0] = (u32x4){0u, 0u, 0u, 0u};
    }
#pragma unroll
    for (int c = 0; c < 4; ++c) {
      unsigned int W[8] = {hw[c][0].x, hw[c][0].y, hw[c][0].z, hw[c][0].w, hw[c][1].x, hw[c][1].y, hw[c][1].z, hw[c][1].w};
      char* hb = lds + c * CV_CH + 16 * (t + 1);
#pragma unroll
      for (int cp = 0; cp < 8; ++cp) {
        unsigned int tt[4];
#pragma unroll
        for (int k = 0; k < 4; ++k) {
          const int rho = cp + 1 + 2 * k;
          if ((rho & 1) == 0) tt[k] = W[rho >> 1];
          else tt[k] = __builtin_amdgcn_alignbit(W[(rho + 1) >> 1], W[(rho - 1) >> 1], 16);
        }
        *reinterpret_cast<u32x4*>(hb + cp * CV_CS) = (u32x4){tt[0], tt[1], tt[2], tt[3]};
      }
    }
  }
  STAMP(2);
  __syncthreads();
  STAMP(3);
  const int ch = wv >> 1, half = wv & 1;
  const int a = lane & 31, hh = lane >> 5;
  const char* hb = lds + ch * CV_CH;
  const char* xb = hb + CV_XOFF;
  const char* ap = hb + (a & 7) * CV_CS + 16 * ((a >> 3) + hh);
  const char* bp = xb + hh * CV_HS + 16 * (a + 32);
  f32x16 acc[2];
#pragma unroll
  for (int i = 0; i < 2; ++i)
#pragma unroll
    for (int j = 0; j < 16; ++j) acc[i][j] = 0.f;
  if (half == 0) conv_mfma<0>(ap, bp, acc); else conv_mfma<1>(ap, bp, acc);
  STAMP(4);
  float* xch = reinterpret_cast<float*>(const_cast<char*>(hb));
  __syncthreads();
  if (a == 31) {
    const int cbl = half ? 1 : 0, cbu = half ? 2 : 3;
#pragma unroll
    for (int i = 0; i < 8; ++i) {
      xch[(cbl * 2 + hh) * 8 + i] = acc[0][8 + i];
      xch[(cbu * 2 + hh) * 8 + i] = acc[1][8 + i];
    }
  }
  __syncthreads();
  {
    const float Dv = bf1(Dp[d0 + ch]);
    u16* zrow = zT + (size_t)(d0 + ch) * S_;
    const int srcl = ((lane & 32) | ((a - 1) & 31)) << 2;
#pragma unroll
    for (int j = 0; j < 2; ++j) {
      const int cb = half ? (j ? 2 : 1) : (j ? 3 : 0);
      const int I = 32 * cb + a;
#pragma unroll
      for (int g = 0; g < 2; ++g) {
        float y[4];
#pragma unroll
        for (int e = 0; e < 4; ++e) {
          const int i = 4 * g + e;
          const float nb = __int_as_float(__builtin_amdgcn_ds_bpermute(srcl, __float_as_int(acc[j][8 + i])));
          const float prevcol = (cb == 0) ? 0.f : xch[((cb - 1) * 2 + hh) * 8 + i];
          y[e] = acc[j][i] + ((a == 0) ? prevcol : nb);
        }
        const u32x2 xr = *reinterpret_cast<const u32x2*>(xb + (g ? 0 : 1) * CV_HS + 16 * (I + 32) + (hh ? 0 : 8));
        const float kv3 = bf_lo(xr.x), kv2 = bf_hi(xr.x), kv1 = bf_lo(xr.y), kv0 = bf_hi(xr.y);
        const float z0 = rbf(y[0]) + rbf(kv0 * Dv), z1 = rbf(y[1]) + rbf(kv1 * Dv);
        const float z2 = rbf(y[2]) + rbf(kv2 * Dv), z3 = rbf(y[3]) + rbf(kv3 * Dv);
        u32x2 o = {pack2(z0, z1), pack2(z2, z3)};
        *reinterpret_cast<u32x2*>(zrow + 16 * I + 8 * g + 4 * hh) = o;
      }
    }
  }
  STAMP(5);
}

static __device__ __forceinline__ float mish_f(float h) {
  const float w = __builtin_amdgcn_exp2f(-fabsf(h) * 1.4426950408889634f);
  const float a = w + w;
  const float p = fmaf(w, w, a);
  const float n2 = a + 1.f;
  const bool pos = h >= 0.f;
  const float num = pos ? n2 : p;
  const float den = pos ? fmaf(a, w, n2) : (p + 2.f);
  return h * num * __builtin_amdgcn_rcpf(den);
}

static __device__ __forceinline__ void gemm1_block(int bid, const u16* __restrict__ xp, const u16* __restrict__ w1,
                                                   const u16* __restrict__ b1p, const u16* __restrict__ gwp,
                                                   u16* __restrict__ act, float* __restrict__ gout, char* lds) {
  const int tid = threadIdx.x, lane = tid & 63, wv = tid >> 6;
  const int xcd = bid & 7, r = bid >> 3;
  const int ti = 2 * xcd + (r & 1), tj = r >> 1;
  const int m0 = 128 * ti;
  const int u0 = (tj <= 3) ? 6 * tj : 18 + 5 * (tj - 3);
  const int nun = (tj < 3) ? 6 : 5;
  const int n0 = 32 * u0;
  const int wr = wv >> 1, wc = wv & 1;

  STAMP(0);
  int aoff[2], boff[3];
#pragma unroll
  for (int c = 0; c < 2; ++c) {
    int o = (wv * 2 + c) * 1024 + 16 * lane;
    int row = o >> 7, slot = (o >> 4) & 7;
    aoff[c] = (m0 + row) * D_ + 8 * (slot ^ ((row >> 1) & 7));
  }
#pragma unroll
  for (int c = 0; c < 3; ++c) {
    int o = (wv * 3 + c) * 1024 + 16 * lane;
    int row = o / 384, slot = (o - row * 384) >> 4;
    int col = n0 + 8 * (slot ^ (((row >> 1) & 1) << 2));
    col = min(col, F_ - 8);
    boff[c] = row * F_ + col;
  }
  const int wvu = __builtin_amdgcn_readfirstlane(wv);
  const unsigned int ldsbase = lds_addr(lds);
  auto issue = [&](int kt) {
    const unsigned int sb = ldsbase + (kt % G1_NST) * G1_STG;
    const u16* xa = xp + 64 * kt;
    const u16* wb = w1 + (size_t)(64 * kt) * F_;
#pragma unroll
    for (int c = 0; c < 2; ++c) glds16(xa + aoff[c], sb + 2048 * wvu + c * 1024);
#pragma unroll
    for (int c = 0; c < 3; ++c) glds16(wb + boff[c], sb + 16384 + 3072 * wvu + c * 1024);
  };
  issue(0);
  issue(1);

  {
    const int row = m0 + 16 * tj + (tid >> 5), part = tid & 31;
    const u16* xr = xp + (size_t)row * D_ + 16 * part;
    const u16* gr = gwp + 16 * part;
    float acc = 0.f;
#pragma unroll
    for (int i = 0; i < 2; ++i) {
      u32x4 xv = *reinterpret_cast<const u32x4*>(xr + 8 * i);
      u32x4 gv = *reinterpret_cast<const u32x4*>(gr + 8 * i);
      acc += rbf(bf_lo(xv.x) * bf_lo(gv.x)) + rbf(bf_hi(xv.x) * bf_hi(gv.x));
      acc += rbf(bf_lo(xv.y) * bf_lo(gv.y)) + rbf(bf_hi(xv.y) * bf_hi(gv.y));
      acc += rbf(bf_lo(xv.z) * bf_lo(gv.z)) + rbf(bf_hi(xv.z) * bf_hi(gv.z));
      acc += rbf(bf_lo(xv.w) * bf_lo(gv.w)) + rbf(bf_hi(xv.w) * bf_hi(gv.w));
    }
#pragma unroll
    for (int o = 16; o > 0; o >>= 1) acc += __shfl_xor(acc, o);
    if (part == 0) {
      float z = rbf(acc);
      gout[row] = rbf(1.f / (1.f + __expf(-z)));
    }
  }

  STAMP(1);
  f32x16 acc[3];
#pragma unroll
  for (int j = 0; j < 3; ++j)
#pragma unroll
    for (int e = 0; e < 16; ++e) acc[j][e] = 0.f;

  const int a31 = lane & 31, hh = lane >> 5;
  const int blk = (lane >> 4) & 1, q4 = (lane & 15) >> 2, p4 = lane & 3;
  const int xrow = 32 * wr + a31;
  const int xoff = xrow * 128, xkey = (xrow >> 1) & 7;

  for (int kt = 0; kt < 8; ++kt) {
    if (kt < 7) { WAIT_VM(5); } else { WAIT_VM(0); }
    __builtin_amdgcn_s_barrier();
    if (kt == 0) STAMP(2);
    if (kt + 2 < 8) issue(kt + 2);
    const char* sa = lds + (kt % G1_NST) * G1_STG;
    const char* sbb = sa + 16384;
    bf16x8 xf[2], wf[2][3];
    auto ldfr = [&](int ks, bf16x8& xd, bf16x8* wd) {
      xd = *reinterpret_cast<const bf16x8*>(sa + xoff + 16 * ((2 * ks + hh) ^ xkey));
#pragma unroll
      for (int uu = 0; uu < 3; ++uu) {
#pragma unroll
        for (int t = 0; t < 2; ++t) {
          const int krow = 16 * ks + 8 * hh + 4 * t + q4;
          const int chunk = 12 * wc + 4 * uu + 2 * blk + (p4 >> 1);
          bf16x4 v4 = tr_read(sbb + krow * 384 + 16 * (chunk ^ (((krow >> 1) & 1) << 2)) + 8 * (p4 & 1));
          wd[uu][4 * t + 0] = v4[0]; wd[uu][4 * t + 1] = v4[1]; wd[uu][4 * t + 2] = v4[2]; wd[uu][4 * t + 3] = v4[3];
        }
      }
    };
    ldfr(0, xf[0], wf[0]);
#pragma unroll
    for (int ks = 0; ks < 4; ++ks) {
      if (ks < 3) ldfr(ks + 1, xf[(ks + 1) & 1], wf[(ks + 1) & 1]);
#pragma unroll
      for (int uu = 0; uu < 3; ++uu)
        acc[uu] = __builtin_amdgcn_mfma_f32_32x32x16_bf16(wf[ks & 1][uu], xf[ks & 1], acc[uu], 0, 0, 0);
    }
  }
  STAMP(4);
  const int m = m0 + xrow;
#pragma unroll
  for (int uu = 0; uu < 3; ++uu) {
    if (3 * wc + uu < nun) {
#pragma unroll
      for (int g4 = 0; g4 < 4; ++g4) {
        const int n = n0 + 96 * wc + 32 * uu + 8 * g4 + 4 * hh;
        u32x2 bv = *reinterpret_cast<const u32x2*>(b1p + n);
        float bb[4] = {bf_lo(bv.x), bf_hi(bv.x), bf_lo(bv.y), bf_hi(bv.y)};
        float o[4];
#pragma unroll
        for (int e = 0; e < 4; ++e) {
          float h = rbf(rbf(acc[uu][4 * g4 + e]) + bb[e]);
          o[e] = mish_f(h);
        }
        u32x2 st = {pack2(o[0], o[1]), pack2(o[2], o[3])};
        *reinterpret_cast<u32x2*>(act + (size_t)m * F_ + n) = st;
      }
    }
  }
  STAMP(5);
}

__global__ __launch_bounds__(512, 1) void k1_kernel(const u16* __restrict__ qx, const u16* __restrict__ kp,
                                                    const u16* __restrict__ vp, const u16* __restrict__ shp,
                                                    const u16* __restrict__ hp, const u16* __restrict__ Dp,
                                                    const u16* __restrict__ w1, const u16* __restrict__ b1p,
                                                    const u16* __restrict__ gwp, char* __restrict__ ws) {
  __shared__ __attribute__((aligned(16))) char lds[K1_LDS];
  const int b = blockIdx.x;
  if (b < 128) {
    const int cg = (b & 7) * 16 + (b >> 3);
    conv_block(cg, kp, vp, shp, hp, Dp, reinterpret_cast<u16*>(ws + WS_ZT), lds);
  } else {
    gemm1_block(b - 128, qx, w1, b1p, gwp, reinterpret_cast<u16*>(ws + WS_ACT), reinterpret_cast<float*>(ws + WS_G), lds);
  }
}

#define G2_STG 16384
#define G2_NST 9
#define G2_NT 22

__global__ __launch_bounds__(256, 1) void k2_kernel(const u16* __restrict__ qx, const u16* __restrict__ w2,
                                                    const char* __restrict__ ws, u16* __restrict__ out) {
  __shared__ __attribute__((aligned(16))) char lds[G2_STG * G2_NST];
  const u16* act = reinterpret_cast<const u16*>(ws + WS_ACT);
  const u16* zT = reinterpret_cast<const u16*>(ws + WS_ZT);
  const float* gp = reinterpret_cast<const float*>(ws + WS_G);
  const int tid = threadIdx.x, lane = tid & 63, wv = tid >> 6;
  const int bid = blockIdx.x;
  const int xcd = bid & 7, r = bid >> 3;
  const int ti = 4 * xcd + (r & 3), tj = r >> 2;
  const int m0 = 64 * ti, n0 = 64 * tj;
  const int wr = wv >> 1, wc = wv & 1;

  int aoff[2], boff[2], arow[2], brow[2];
#pragma unroll
  for (int c = 0; c < 2; ++c) {
    int o = (wv * 2 + c) * 1024 + 16 * lane;
    int row = o >> 7, slot = (o >> 4) & 7;
    arow[c] = (m0 + row) * F_;
    aoff[c] = 8 * (slot ^ ((row >> 1) & 7));
    brow[c] = row;
    boff[c] = n0 + 8 * (slot ^ (((row >> 1) & 1) << 2));
  }
  const int wvu = __builtin_amdgcn_readfirstlane(wv);
  const unsigned int ldsbase = lds_addr(lds) + 2048 * wvu;
  auto issue = [&](int kt) {
    const unsigned int sb = ldsbase + (kt % G2_NST) * G2_STG;
    const int k0 = 64 * kt;
#pragma unroll
    for (int c = 0; c < 2; ++c) {
      int kc = min(k0 + aoff[c], F_ - 8);
      glds16(act + arow[c] + kc, sb + c * 1024);
    }
#pragma unroll
    for (int c = 0; c < 2; ++c) {
      int kr = min(k0 + brow[c], F_ - 1);
      glds16(w2 + (size_t)kr * D_ + boff[c], sb + 8192 + c * 1024);
    }
  };
#pragma unroll
  for (int i = 0; i < G2_NST - 1; ++i) issue(i);

  f32x16 acc;
#pragma unroll
  for (int e = 0; e < 16; ++e) acc[e] = 0.f;
  const int a31 = lane & 31, hh = lane >> 5;
  const int blk = (lane >> 4) & 1, q4 = (lane & 15) >> 2, p4 = lane & 3;
  const int xrow = 32 * wr + a31;
  const int xoff = xrow * 128, xkey = (xrow >> 1) & 7;

  for (int kt = 0; kt < G2_NT; ++kt) {
    if (kt < G2_NT - (G2_NST - 2)) { WAIT_VM(28); } else { WAIT_VM(0); }
    __builtin_amdgcn_s_barrier();
    if (kt + G2_NST - 1 < G2_NT) issue(kt + G2_NST - 1);
    const char* sa = lds + (kt % G2_NST) * G2_STG;
    const char* sbb = sa + 8192;
    const int nks = (kt == G2_NT - 1) ? 2 : 4;
#pragma unroll
    for (int ks = 0; ks < 4; ++ks) {
      if (ks < nks) {
        bf16x8 xf = *reinterpret_cast<const bf16x8*>(sa + xoff + 16 * ((2 * ks + hh) ^ xkey));
        bf16x8 wf;
#pragma unroll
        for (int t = 0; t < 2; ++t) {
          const int krow = 16 * ks + 8 * hh + 4 * t + q4;
          const int chunk = 4 * wc + 2 * blk + (p4 >> 1);
          bf16x4 v4 = tr_read(sbb + krow * 128 + 16 * (chunk ^ (((krow >> 1) & 1) << 2)) + 8 * (p4 & 1));
          wf[4 * t + 0] = v4[0]; wf[4 * t + 1] = v4[1]; wf[4 * t + 2] = v4[2]; wf[4 * t + 3] = v4[3];
        }
        acc = __builtin_amdgcn_mfma_f32_32x32x16_bf16(wf, xf, acc, 0, 0, 0);
      }
    }
  }
  const int s = m0 + xrow;
  const float g = gp[s];
  const float omg = rbf(1.f - g);
#pragma unroll
  for (int g4 = 0; g4 < 4; ++g4) {
    const int d = n0 + 32 * wc + 8 * g4 + 4 * hh;
    u32x2 qv = *reinterpret_cast<const u32x2*>(qx + (size_t)s * D_ + d);
    float qf[4] = {bf_lo(qv.x), bf_hi(qv.x), bf_lo(qv.y), bf_hi(qv.y)};
    float o[4];
#pragma unroll
    for (int e = 0; e < 4; ++e) {
      float z = bf1(zT[(size_t)(d + e) * S_ + s]);
      float av = rbf(qf[e] * z);
      float bv = rbf(acc[4 * g4 + e]);
      o[e] = rbf(g * av) + rbf(omg * bv);
    }
    u32x2 st = {pack2(o[0], o[1]), pack2(o[2], o[3])};
    *reinterpret_cast<u32x2*>(out + (size_t)s * D_ + d) = st;
  }
}

extern "C" void kernel_launch(void* const* d_in, const int* in_sizes, int n_in,
                              void* d_out, int out_size, void* d_ws, size_t ws_size,
                              hipStream_t stream) {
  const u16* qx = (const u16*)d_in[0];
  const u16* kp = (const u16*)d_in[1];
  const u16* vp = (const u16*)d_in[2];
  const u16* shp = (const u16*)d_in[3];
  const u16* hp = (const u16*)d_in[4];
  const u16* Dp = (const u16*)d_in[5];
  const u16* w1 = (const u16*)d_in[7];
  const u16* b1p = (const u16*)d_in[8];
  const u16* w2 = (const u16*)d_in[9];
  const u16* gwp = (const u16*)d_in[10];
  k1_kernel<<<256, 512, 0, stream>>>(qx, kp, vp, shp, hp, Dp, w1, b1p, gwp, (char*)d_ws);
  k2_kernel<<<256, 256, 0, stream>>>(qx, w2, (const char*)d_ws, (u16*)d_out);
}
```

```cpp
#include <hip/hip_runtime.h>
#include <hip/hip_bf16.h>
#include <stdint.h>

#define S_ 2048
#define D_ 512
#define F_ 1376

typedef __attribute__((ext_vector_type(8))) short bf16x8;
typedef __attribute__((ext_vector_type(4))) short bf16x4;
typedef __attribute__((ext_vector_type(16))) float f32x16;
typedef __attribute__((ext_vector_type(4))) unsigned int u32x4;
typedef __attribute__((ext_vector_type(2))) unsigned int u32x2;
typedef unsigned short u16;

static __device__ __forceinline__ float bf_lo(unsigned int w) { return __uint_as_float(w << 16); }
static __device__ __forceinline__ float bf_hi(unsigned int w) { return __uint_as_float(w & 0xffff0000u); }
static __device__ __forceinline__ float bf1(u16 b) { return __uint_as_float(((unsigned int)b) << 16); }
static __device__ __forceinline__ u16 f2bf(float x) {
  __hip_bfloat16 b = __float2bfloat16(x);
  return *reinterpret_cast<u16*>(&b);
}
static __device__ __forceinline__ float rbf(float x) { return bf1(f2bf(x)); }
static __device__ __forceinline__ unsigned int pack2(float lo, float hi) {
  return (unsigned int)f2bf(lo) | (((unsigned int)f2bf(hi)) << 16);
}

#define STAMP2(i)
#define STAMP(i)
#define WAIT_VM(n) asm volatile("s_waitcnt vmcnt(" #n ")" ::: "memory")
#define WAIT_LGKM0() asm volatile("s_waitcnt lgkmcnt(0)" ::: "memory")

static __device__ __forceinline__ void glds16(const void* g, unsigned int lds_addr_uniform) {
  unsigned int keep;
  asm volatile("s_mov_b32 %0, m0\n\ts_mov_b32 m0, %2\n\ts_nop 0\n\tglobal_load_lds_dwordx4 %1, off\n\ts_mov_b32 m0, %0"
               : "=&s"(keep) : "v"(g), "s"(lds_addr_uniform) : "memory");
}
static __device__ __forceinline__ unsigned int lds_addr(const void* p) { return (unsigned int)(size_t)p; }
static __device__ __forceinline__ bf16x4 tr_read(const char* p) {
  return __builtin_amdgcn_ds_read_tr16_b64_v4i16(
      (__attribute__((address_space(3))) bf16x4*)(p));
}

#define CV_CS 4160
#define CV_HS 2624
#define CV_XOFF (8 * CV_CS)
#define CV_CH (CV_XOFF + 2 * CV_HS)
#define K1_LDS (4 * CV_CH)
#define G1_STG 40960
#define G1_NST 3

#define WS_ACT 0
#define WS_ZT (WS_ACT + (size_t)S_ * F_ * 2)
#define WS_G (WS_ZT + (size_t)D_ * S_ * 2)

template <int HALF>
static __device__ __forceinline__ void conv_mfma(const char* ap, const char* bp, f32x16* acc) {
  constexpr int CB0 = HALF ? 1 : 0, CB1 = HALF ? 2 : 3;
  constexpr int Q0 = 16 * (CB0 + 1), Q1 = 16 * (CB1 + 1);
  bf16x8 An = *reinterpret_cast<const bf16x8*>(ap);
  bf16x8 B0n = *reinterpret_cast<const bf16x8*>(bp + 512 * CB0);
  bf16x8 B1n = *reinterpret_cast<const bf16x8*>(bp + 512 * CB1);
#pragma unroll
  for (int q = 0; q < Q1; ++q) {
    const bf16x8 Ac = An, B0c = B0n, B1c = B1n;
    if (q + 1 < Q1) {
      An = *reinterpret_cast<const bf16x8*>(ap + 64 * (q + 1));
      if (q + 1 < Q0) B0n = *reinterpret_cast<const bf16x8*>(bp + 512 * CB0 - 32 * (q + 1));
      B1n = *reinterpret_cast<const bf16x8*>(bp + 512 * CB1 - 32 * (q + 1));
    }
    if (q < Q0) acc[0] = __builtin_amdgcn_mfma_f32_32x32x16_bf16(Ac, B0c, acc[0], 0, 0, 0);
    acc[1] = __builtin_amdgcn_mfma_f32_32x32x16_bf16(Ac, B1c, acc[1], 0, 0, 0);
  }
}

static __device__ __forceinline__ void conv_block(int cg, const u16* __restrict__ kp, const u16* __restrict__ vp,
                                                  const u16* __restrict__ shp, const u16* __restrict__ hp,
                                                  const u16* __restrict__ Dp, u16* __restrict__ zT, char* lds) {
  const int tid = threadIdx.x, lane = tid & 63, wv = tid >> 6;
  const int d0 = cg * 4;
  STAMP(0);
  if (tid < 256) {
    u32x2 kr[11], vr[8];
#pragma unroll
    for (int i = 0; i < 11; ++i) {
      const int row = max(8 * tid - 3 + i, 0);
      kr[i] = *reinterpret_cast<const u32x2*>(kp + (size_t)row * D_ + d0);
    }
#pragma unroll
    for (int i = 0; i < 8; ++i) vr[i] = *reinterpret_cast<const u32x2*>(vp + (size_t)(8 * tid + i) * D_ + d0);
    float sh[4][4];
    {
      u32x4 s0 = *reinterpret_cast<const u32x4*>(shp + d0 * 4);
      u32x4 s1 = *reinterpret_cast<const u32x4*>(shp + d0 * 4 + 8);
      sh[0][0] = bf_lo(s0.x); sh[0][1] = bf_hi(s0.x); sh[0][2] = bf_lo(s0.y); sh[0][3] = bf_hi(s0.y);
      sh[1][0] = bf_lo(s0.z); sh[1][1] = bf_hi(s0.z); sh[1][2] = bf_lo(s0.w); sh[1][3] = bf_hi(s0.w);
      sh[2][0] = bf_lo(s1.x); sh[2][1] = bf_hi(s1.x); sh[2][2] = bf_lo(s1.y); sh[2][3] = bf_hi(s1.y);
      sh[3][0] = bf_lo(s1.z); sh[3][1] = bf_hi(s1.z); sh[3][2] = bf_lo(s1.w); sh[3][3] = bf_hi(s1.w);
    }
    {
      const int c = tid >> 6, idx = tid & 63;
      *reinterpret_cast<u32x4*>(lds + c * CV_CH + CV_XOFF + (idx >> 5) * CV_HS + 16 * (idx & 31)) = (u32x4){0u, 0u, 0u, 0u};
    }
    if (tid == 0) {
#pragma unroll
      for (int i = 0; i < 3; ++i) kr[i] = (u32x2){0u, 0u};
    }
    float kf[11][4];
#pragma unroll
    for (int i = 0; i < 11; ++i) { kf[i][0] = bf_lo(kr[i].x); kf[i][1] = bf_hi(kr[i].x); kf[i][2] = bf_lo(kr[i].y); kf[i][3] = bf_hi(kr[i].y); }
    const int Ip = tid >> 1, hsel = (tid & 1) ? 0 : 1;
#pragma unroll
    for (int c = 0; c < 4; ++c) {
      u16 xs[8];
#pragma unroll
      for (int i = 0; i < 8; ++i) {
        float ks = kf[i][c] * sh[c][3];
        ks = fmaf(kf[i + 1][c], sh[c][2], ks);
        ks = fmaf(kf[i + 2][c], sh[c][1], ks);
        ks = fmaf(kf[i + 3][c], sh[c][0], ks);
        float vv = (c == 0) ? bf_lo(vr[i].x) : (c == 1) ? bf_hi(vr[i].x) : (c == 2) ? bf_lo(vr[i].y) : bf_hi(vr[i].y);
        xs[i] = f2bf(rbf(ks) * vv);
      }
      u32x4 o;
      o.x = (unsigned)xs[7] | ((unsigned)xs[6] << 16);
      o.y = (unsigned)xs[5] | ((unsigned)xs[4] << 16);
      o.z = (unsigned)xs[3] | ((unsigned)xs[2] << 16);
      o.w = (unsigned)xs[1] | ((unsigned)xs[0] << 16);
      *reinterpret_cast<u32x4*>(lds + c * CV_CH + CV_XOFF + hsel * CV_HS + 16 * (Ip + 32)) = o;
    }
  } else {
    const int t = tid - 256;
    u32x4 hw[4][2];
#pragma unroll
    for (int c = 0; c < 4; ++c) {
      const u16* hrow = hp + (size_t)(d0 + c) * S_;
      hw[c][0] = *reinterpret_cast<const u32x4*>(hrow + 8 * max(t - 1, 0));
      hw[c][1] = *reinterpret_cast<const u32x4*>(hrow + 8 * t);
    }
    if (t < 32) *reinterpret_cast<u32x4*>(lds + (t >> 3) * CV_CH + (t & 7) * CV_CS) = (u32x4){0u, 0u, 0u, 0u};
    if (t == 0) {
#pragma unroll
      for (int c = 0; c < 4; ++c) hw[c][0] = (u32x4){0u, 0u, 0u, 0u};
    }
#pragma unroll
    for (int c = 0; c < 4; ++c) {
      unsigned int W[8] = {hw[c][0].x, hw[c][0].y, hw[c][0].z, hw[c][0].w, hw[c][1].x, hw[c][1].y, hw[c][1].z, hw[c][1].w};
      char* hb = lds + c * CV_CH + 16 * (t + 1);
#pragma unroll
      for (int cp = 0; cp < 8; ++cp) {
        unsigned int tt[4];
#pragma unroll
        for (int k = 0; k < 4; ++k) {
          const int rho = cp + 1 + 2 * k;
          if ((rho & 1) == 0) tt[k] = W[rho >> 1];
          else tt[k] = __builtin_amdgcn_alignbit(W[(rho + 1) >> 1], W[(rho - 1) >> 1], 16);
        }
        *reinterpret_cast<u32x4*>(hb + cp * CV_CS) = (u32x4){tt[0], tt[1], tt[2], tt[3]};
      }
    }
  }
  STAMP(2);
  __syncthreads();
  STAMP(3);
  const int ch = wv >> 1, half = wv & 1;
  const int a = lane & 31, hh = lane >> 5;
  const char* hb = lds + ch * CV_CH;
  const char* xb = hb + CV_XOFF;
  const char* ap = hb + (a & 7) * CV_CS + 16 * ((a >> 3) + hh);
  const char* bp = xb + hh * CV_HS + 16 * (a + 32);
  f32x16 acc[2];
#pragma unroll
  for (int i = 0; i < 2; ++i)
#pragma unroll
    for (int j = 0; j < 16; ++j) acc[i][j] = 0.f;
  if (half == 0) conv_mfma<0>(ap, bp, acc); else conv_mfma<1>(ap, bp, acc);
  STAMP(4);
  float* xch = reinterpret_cast<float*>(const_cast<char*>(hb));
  __syncthreads();
  if (a == 31) {
    const int cbl = half ? 1 : 0, cbu = half ? 2 : 3;
#pragma unroll
    for (int i = 0; i < 8; ++i) {
      xch[(cbl * 2 + hh) * 8 + i] = acc[0][8 + i];
      xch[(cbu * 2 + hh) * 8 + i] = acc[1][8 + i];
    }
  }
  __syncthreads();
  {
    const float Dv = bf1(Dp[d0 + ch]);
    u16* zrow = zT + (size_t)(d0 + ch) * S_;
    const int srcl = ((lane & 32) | ((a - 1) & 31)) << 2;
#pragma unroll
    for (int j = 0; j < 2; ++j) {
      const int cb = half ? (j ? 2 : 1) : (j ? 3 : 0);
      const int I = 32 * cb + a;
#pragma unroll
      for (int g = 0; g < 2; ++g) {
        float y[4];
#pragma unroll
        for (int e = 0; e < 4; ++e) {
          const int i = 4 * g + e;
          const float nb = __int_as_float(__builtin_amdgcn_ds_bpermute(srcl, __float_as_int(acc[j][8 + i])));
          const float prevcol = (cb == 0) ? 0.f : xch[((cb - 1) * 2 + hh) * 8 + i];
          y[e] = acc[j][i] + ((a == 0) ? prevcol : nb);
        }
        const u32x2 xr = *reinterpret_cast<const u32x2*>(xb + (g ? 0 : 1) * CV_HS + 16 * (I + 32) + (hh ? 0 : 8));
        const float kv3 = bf_lo(xr.x), kv2 = bf_hi(xr.x), kv1 = bf_lo(xr.y), kv0 = bf_hi(xr.y);
        const float z0 = rbf(y[0]) + rbf(kv0 * Dv), z1 = rbf(y[1]) + rbf(kv1 * Dv);
        const float z2 = rbf(y[2]) + rbf(kv2 * Dv), z3 = rbf(y[3]) + rbf(kv3 * Dv);
        u32x2 o = {pack2(z0, z1), pack2(z2, z3)};
        *reinterpret_cast<u32x2*>(zrow + 16 * I + 8 * g + 4 * hh) = o;
      }
    }
  }
  STAMP(5);
}

static __device__ __forceinline__ float mish_f(float h) {
  const float w = __builtin_amdgcn_exp2f(-fabsf(h) * 1.4426950408889634f);
  const float a = w + w;
  const float p = fmaf(w, w, a);
  const float n2 = a + 1.f;
  const bool pos = h >= 0.f;
  const float num = pos ? n2 : p;
  const float den = pos ? fmaf(a, w, n2) : (p + 2.f);
  return h * num * __builtin_amdgcn_rcpf(den);
}

static __device__ __forceinline__ void gemm1_block(int bid, const u16* __restrict__ xp, const u16* __restrict__ w1,
                                                   const u16* __restrict__ b1p, const u16* __restrict__ gwp,
                                                   u16* __restrict__ act, float* __restrict__ gout, char* lds) {
  const int tid = threadIdx.x, lane = tid & 63, wv = tid >> 6;
  const int xcd = bid & 7, r = bid >> 3;
  const int ti = 2 * xcd + (r & 1), tj = r >> 1;
  const int m0 = 128 * ti;
  const int u0 = (tj <= 3) ? 6 * tj : 18 + 5 * (tj - 3);
  const int nun = (tj < 3) ? 6 : 5;
  const int n0 = 32 * u0;
  const int wr = wv >> 1, wc = wv & 1;

  STAMP(0);
  int aoff[2], boff[3];
#pragma unroll
  for (int c = 0; c < 2; ++c) {
    int o = (wv * 2 + c) * 1024 + 16 * lane;
    int row = o >> 7, slot = (o >> 4) & 7;
    aoff[c] = (m0 + row) * D_ + 8 * (slot ^ ((row >> 1) & 7));
  }
#pragma unroll
  for (int c = 0; c < 3; ++c) {
    int o = (wv * 3 + c) * 1024 + 16 * lane;
    int row = o / 384, slot = (o - row * 384) >> 4;
    int col = n0 + 8 * (slot ^ (((row >> 1) & 1) << 2));
    col = min(col, F_ - 8);
    boff[c] = row * F_ + col;
  }
  const int wvu = __builtin_amdgcn_readfirstlane(wv);
  const unsigned int ldsbase = lds_addr(lds);
  auto issue = [&](int kt) {
    const unsigned int sb = ldsbase + (kt % G1_NST) * G1_STG;
    const u16* xa = xp + 64 * kt;
    const u16* wb = w1 + (size_t)(64 * kt) * F_;
#pragma unroll
    for (int c = 0; c < 2; ++c) glds16(xa + aoff[c], sb + 2048 * wvu + c * 1024);
#pragma unroll
    for (int c = 0; c < 3; ++c) glds16(wb + boff[c], sb + 16384 + 3072 * wvu + c * 1024);
  };
  issue(0);
  issue(1);

  {
    const int row = m0 + 16 * tj + (tid >> 5), part = tid & 31;
    const u16* xr = xp + (size_t)row * D_ + 16 * part;
    const u16* gr = gwp + 16 * part;
    float acc = 0.f;
#pragma unroll
    for (int i = 0; i < 2; ++i) {
      u32x4 xv = *reinterpret_cast<const u32x4*>(xr + 8 * i);
      u32x4 gv = *reinterpret_cast<const u32x4*>(gr + 8 * i);
      acc += rbf(bf_lo(xv.x) * bf_lo(gv.x)) + rbf(bf_hi(xv.x) * bf_hi(gv.x));
      acc += rbf(bf_lo(xv.y) * bf_lo(gv.y)) + rbf(bf_hi(xv.y) * bf_hi(gv.y));
      acc += rbf(bf_lo(xv.z) * bf_lo(gv.z)) + rbf(bf_hi(xv.z) * bf_hi(gv.z));
      acc += rbf(bf_lo(xv.w) * bf_lo(gv.w)) + rbf(bf_hi(xv.w) * bf_hi(gv.w));
    }
#pragma unroll
    for (int o = 16; o > 0; o >>= 1) acc += __shfl_xor(acc, o);
    if (part == 0) {
      float z = rbf(acc);
      gout[row] = rbf(1.f / (1.f + __expf(-z)));
    }
  }

  STAMP(1);
  f32x16 acc[3];
#pragma unroll
  for (int j = 0; j < 3; ++j)
#pragma unroll
    for (int e = 0; e < 16; ++e) acc[j][e] = 0.f;

  const int a31 = lane & 31, hh = lane >> 5;
  const int blk = (lane >> 4) & 1, q4 = (lane & 15) >> 2, p4 = lane & 3;
  const int xrow = 32 * wr + a31;
  const int xoff = xrow * 128, xkey = (xrow >> 1) & 7;

  for (int kt = 0; kt < 8; ++kt) {
    if (kt < 7) { WAIT_VM(5); } else { WAIT_VM(0); }
    __builtin_amdgcn_s_barrier();
    if (kt == 0) STAMP(2);
    if (kt + 2 < 8) issue(kt + 2);
    const char* sa = lds + (kt % G1_NST) * G1_STG;
    const char* sbb = sa + 16384;
    bf16x8 xf[2], wf[2][3];
    auto ldfr = [&](int ks, bf16x8& xd, bf16x8* wd) {
      xd = *reinterpret_cast<const bf16x8*>(sa + xoff + 16 * ((2 * ks + hh) ^ xkey));
#pragma unroll
      for (int uu = 0; uu < 3; ++uu) {
#pragma unroll
        for (int t = 0; t < 2; ++t) {
          const int krow = 16 * ks + 8 * hh + 4 * t + q4;
          const int chunk = 12 * wc + 4 * uu + 2 * blk + (p4 >> 1);
          bf16x4 v4 = tr_read(sbb + krow * 384 + 16 * (chunk ^ (((krow >> 1) & 1) << 2)) + 8 * (p4 & 1));
          wd[uu][4 * t + 0] = v4[0]; wd[uu][4 * t + 1] = v4[1]; wd[uu][4 * t + 2] = v4[2]; wd[uu][4 * t + 3] = v4[3];
        }
      }
    };
    ldfr(0, xf[0], wf[0]);
#pragma unroll
    for (int ks = 0; ks < 4; ++ks) {
      if (ks < 3) ldfr(ks + 1, xf[(ks + 1) & 1], wf[(ks + 1) & 1]);
#pragma unroll
      for (int uu = 0; uu < 3; ++uu)
        acc[uu] = __builtin_amdgcn_mfma_f32_32x32x16_bf16(wf[ks & 1][uu], xf[ks & 1], acc[uu], 0, 0, 0);
    }
  }
  STAMP(4);
  const int m = m0 + xrow;
#pragma unroll
  for (int uu = 0; uu < 3; ++uu) {
    if (3 * wc + uu < nun) {
#pragma unroll
      for (int g4 = 0; g4 < 4; ++g4) {
        const int n = n0 + 96 * wc + 32 * uu + 8 * g4 + 4 * hh;
        u32x2 bv = *reinterpret_cast<const u32x2*>(b1p + n);
        float bb[4] = {bf_lo(bv.x), bf_hi(bv.x), bf_lo(bv.y), bf_hi(bv.y)};
        float o[4];
#pragma unroll
        for (int e = 0; e < 4; ++e) {
          float h = rbf(rbf(acc[uu][4 * g4 + e]) + bb[e]);
          o[e] = mish_f(h);
        }
        u32x2 st = {pack2(o[0], o[1]), pack2(o[2], o[3])};
        *reinterpret_cast<u32x2*>(act + (size_t)m * F_ + n) = st;
      }
    }
  }
  STAMP(5);
}

__global__ __launch_bounds__(512, 1) void k1_kernel(const u16* __restrict__ qx, const u16* __restrict__ kp,
                                                    const u16* __restrict__ vp, const u16* __restrict__ shp,
                                                    const u16* __restrict__ hp, const u16* __restrict__ Dp,
                                                    const u16* __restrict__ w1, const u16* __restrict__ b1p,
                                                    const u16* __restrict__ gwp, char* __restrict__ ws) {
  __shared__ __attribute__((aligned(16))) char lds[K1_LDS];
  const int b = blockIdx.x;
  if (b < 128) {
    const int cg = (b & 7) * 16 + (b >> 3);
    conv_block(cg, kp, vp, shp, hp, Dp, reinterpret_cast<u16*>(ws + WS_ZT), lds);
  } else {
    gemm1_block(b - 128, qx, w1, b1p, gwp, reinterpret_cast<u16*>(ws + WS_ACT), reinterpret_cast<float*>(ws + WS_G), lds);
  }
}

#define G2_STG 16384
#define G2_NST 9
#define G2_NT 22

__global__ __launch_bounds__(256, 1) void k2_kernel(const u16* __restrict__ qx, const u16* __restrict__ w2,
                                                    const char* __restrict__ ws, u16* __restrict__ out) {
  __shared__ __attribute__((aligned(16))) char lds[G2_STG * G2_NST];
  const u16* act = reinterpret_cast<const u16*>(ws + WS_ACT);
  const u16* zT = reinterpret_cast<const u16*>(ws + WS_ZT);
  const float* gp = reinterpret_cast<const float*>(ws + WS_G);
  const int tid = threadIdx.x, lane = tid & 63, wv = tid >> 6;
  const int bid = blockIdx.x;
  const int xcd = bid & 7, r = bid >> 3;
  const int ti = 4 * xcd + (r & 3), tj = r >> 2;
  const int m0 = 64 * ti, n0 = 64 * tj;
  const int wr = wv >> 1, wc = wv & 1;

  STAMP2(0);
  int aoff[2], boff[2], arow[2], brow[2];
#pragma unroll
  for (int c = 0; c < 2; ++c) {
    int o = (wv * 2 + c) * 1024 + 16 * lane;
    int row = o >> 7, slot = (o >> 4) & 7;
    arow[c] = (m0 + row) * F_;
    aoff[c] = 8 * (slot ^ ((row >> 1) & 7));
    brow[c] = row;
    boff[c] = n0 + 8 * (slot ^ (((row >> 1) & 1) << 2));
  }
  const int wvu = __builtin_amdgcn_readfirstlane(wv);
  const unsigned int ldsbase = lds_addr(lds) + 2048 * wvu;
  auto issue = [&](int kt) {
    const unsigned int sb = ldsbase + (kt % G2_NST) * G2_STG;
    const int k0 = 64 * kt;
#pragma unroll
    for (int c = 0; c < 2; ++c) {
      int kc = min(k0 + aoff[c], F_ - 8);
      glds16(act + arow[c] + kc, sb + c * 1024);
    }
#pragma unroll
    for (int c = 0; c < 2; ++c) {
      int kr = min(k0 + brow[c], F_ - 1);
      glds16(w2 + (size_t)kr * D_ + boff[c], sb + 8192 + c * 1024);
    }
  };
  const int a31p = lane & 31, hhp = lane >> 5;
  const int srow = m0 + 32 * wr + a31p;
  u32x2 qpre[4];
  u16 zpre[16];
#pragma unroll
  for (int g4 = 0; g4 < 4; ++g4) {
    const int d = n0 + 32 * wc + 8 * g4 + 4 * hhp;
    qpre[g4] = *reinterpret_cast<const u32x2*>(qx + (size_t)srow * D_ + d);
#pragma unroll
    for (int e = 0; e < 4; ++e) zpre[4 * g4 + e] = zT[(size_t)(d + e) * S_ + srow];
  }
  const float gpre = gp[srow];
#pragma unroll
  for (int i = 0; i < G2_NST - 1; ++i) issue(i);

  f32x16 acc;
#pragma unroll
  for (int e = 0; e < 16; ++e) acc[e] = 0.f;
  const int a31 = lane & 31, hh = lane >> 5;
  const int blk = (lane >> 4) & 1, q4 = (lane & 15) >> 2, p4 = lane & 3;
  const int xrow = 32 * wr + a31;
  const int xoff = xrow * 128, xkey = (xrow >> 1) & 7;

  for (int kt = 0; kt < G2_NT; ++kt) {
    if (kt < G2_NT - (G2_NST - 2)) { WAIT_VM(28); } else { WAIT_VM(0); }
    __builtin_amdgcn_s_barrier();
    if (kt == 0) STAMP2(1);
    if (kt == 8) STAMP2(2);
    if (kt == 16) STAMP2(3);
    if (kt + G2_NST - 1 < G2_NT) issue(kt + G2_NST - 1);
    const char* sa = lds + (kt % G2_NST) * G2_STG;
    const char* sbb = sa + 8192;
    const int nks = (kt == G2_NT - 1) ? 2 : 4;
#pragma unroll
    for (int ks = 0; ks < 4; ++ks) {
      if (ks < nks) {
        bf16x8 xf = *reinterpret_cast<const bf16x8*>(sa + xoff + 16 * ((2 * ks + hh) ^ xkey));
        bf16x8 wf;
#pragma unroll
        for (int t = 0; t < 2; ++t) {
          const int krow = 16 * ks + 8 * hh + 4 * t + q4;
          const int chunk = 4 * wc + 2 * blk + (p4 >> 1);
          bf16x4 v4 = tr_read(sbb + krow * 128 + 16 * (chunk ^ (((krow >> 1) & 1) << 2)) + 8 * (p4 & 1));
          wf[4 * t + 0] = v4[0]; wf[4 * t + 1] = v4[1]; wf[4 * t + 2] = v4[2]; wf[4 * t + 3] = v4[3];
        }
        acc = __builtin_amdgcn_mfma_f32_32x32x16_bf16(wf, xf, acc, 0, 0, 0);
      }
    }
  }
  STAMP2(4);
  const int s = m0 + xrow;
  const float g = gpre;
  const float omg = rbf(1.f - g);
#pragma unroll
  for (int g4 = 0; g4 < 4; ++g4) {
    const int d = n0 + 32 * wc + 8 * g4 + 4 * hh;
    const u32x2 qv = qpre[g4];
    float qf[4] = {bf_lo(qv.x), bf_hi(qv.x), bf_lo(qv.y), bf_hi(qv.y)};
    float o[4];
#pragma unroll
    for (int e = 0; e < 4; ++e) {
      float z = bf1(zpre[4 * g4 + e]);
      float av = rbf(qf[e] * z);
      float bv = rbf(acc[4 * g4 + e]);
      o[e] = rbf(g * av) + rbf(omg * bv);
    }
    u32x2 st = {pack2(o[0], o[1]), pack2(o[2], o[3])};
    *reinterpret_cast<u32x2*>(out + (size_t)s * D_ + d) = st;
  }
  STAMP2(5);
}

extern "C" void kernel_launch(void* const* d_in, const int* in_sizes, int n_in,
                              void* d_out, int out_size, void* d_ws, size_t ws_size,
                              hipStream_t stream) {
  const u16* qx = (const u16*)d_in[0];
  const u16* kp = (const u16*)d_in[1];
  const u16* vp = (const u16*)d_in[2];
  const u16* shp = (const u16*)d_in[3];
  const u16* hp = (const u16*)d_in[4];
  const u16* Dp = (const u16*)d_in[5];
  const u16* w1 = (const u16*)d_in[7];
  const u16* b1p = (const u16*)d_in[8];
  const u16* w2 = (const u16*)d_in[9];
  const u16* gwp = (const u16*)d_in[10];
  k1_kernel<<<256, 512, 0, stream>>>(qx, kp, vp, shp, hp, Dp, w1, b1p, gwp, (char*)d_ws);
  k2_kernel<<<256, 256, 0, stream>>>(qx, w2, (const char*)d_ws, (u16*)d_out);
}
```
